```python
import torch

def _softmax(x, axis=-1):
    return torch.softmax(x, dim=axis)
def _gelu(x):
    return torch.nn.functional.gelu(x, approximate="tanh")
def _silu(x):
    return torch.nn.functional.silu(x)
def _sigmoid(x):
    return torch.sigmoid(x)
def _layernorm(x, axis=-1, eps=1e-5):
    m = x.mean(dim=axis, keepdim=True)
    v = x.var(dim=axis, keepdim=True, unbiased=False)
    return (x - m) / torch.sqrt(v + eps)
def _scatter_add(tgt, idx, src, axis=0):
    return tgt.clone().index_add_(axis, idx, src)
def _segment_sum(src, seg, n):
    out = torch.zeros((n,) + src.shape[1:], dtype=src.dtype)
    return out.index_add_(0, seg, src)
def _pad(x, pw):
    flat = tuple(v for pair in reversed(pw) for v in pair)
    return torch.nn.functional.pad(x, flat)

kernel_name = 'concat_heads__sliding_block_cached__sliding_block_cached__decode'

def setup_inputs():

    _w = (lambda a: (lambda t: t.to(torch.bfloat16) if t.is_floating_point() else t)(torch.as_tensor(a)))
    def _A_setup():
        B, Sq, D = 8, 1, 512
        Hq, Hkv, Sk, Dh = 32, 8, 4096, 128
        assert Hq % Hkv == 0, (Hq, Hkv)
        return {
            "x": _w((lambda *s: torch.randn(*s, dtype=torch.bfloat16) * 0.8)(B, Sq, D)),
            "k_cache": _w((lambda *s: torch.randn(*s, dtype=torch.bfloat16) * 0.8)(B, Hkv, Sk, Dh)),
            "v_cache": _w((lambda *s: torch.randn(*s, dtype=torch.bfloat16) * 0.05)(B, Hkv, Sk, Dh)),
            "wq": _w((lambda *s: torch.randn(*s, dtype=torch.bfloat16) * 0.8)(D, Hq * Dh) * (D ** -0.5)),
            "wo": _w((lambda *s: torch.randn(*s, dtype=torch.bfloat16) * 0.05)(Hq * Dh, D) * ((Hq * Dh) ** -0.5)),
        }
    _A_in = _A_setup()

    def _B_setup():
        B, Sq, D = 8, 1, 512
        Hq, Hkv, Sk, Dh = 32, 8, 4096, 128
        assert Hq % Hkv == 0, (Hq, Hkv)
        return {
            "x": _w((lambda *s: torch.randn(*s, dtype=torch.bfloat16) * 0.8)(B, Sq, D)),
            "k_cache": _w((lambda *s: torch.randn(*s, dtype=torch.bfloat16) * 0.8)(B, Hkv, Sk, Dh)),
            "v_cache": _w((lambda *s: torch.randn(*s, dtype=torch.bfloat16) * 0.05)(B, Hkv, Sk, Dh)),
            "wq": _w((lambda *s: torch.randn(*s, dtype=torch.bfloat16) * 0.8)(D, Hq * Dh) * (D ** -0.5)),
            "wo": _w((lambda *s: torch.randn(*s, dtype=torch.bfloat16) * 0.05)(Hq * Dh, D) * ((Hq * Dh) ** -0.5)),
        }
    _B_in = _B_setup()

    return {
        "A_x": _A_in["x"],
        "A_k_cache": _A_in["k_cache"],
        "A_v_cache": _A_in["v_cache"],
        "A_wq": _A_in["wq"],
        "A_wo": _A_in["wo"],
        "B_x": _B_in["x"],
        "B_k_cache": _B_in["k_cache"],
        "B_v_cache": _B_in["v_cache"],
        "B_wq": _B_in["wq"],
        "B_wo": _B_in["wo"],
    }

def reference(A_x, A_k_cache, A_v_cache, A_wq, A_wo, B_x, B_k_cache, B_v_cache, B_wq, B_wo):
    def _A_ref(x, k_cache, v_cache, wq, wo):
        B, Sq, D = x.shape
        Hq, Dh = 32, 128
        Hkv, Sk = k_cache.shape[1], k_cache.shape[2]
        W = min(4096, max(256, Sk // 4))
        rep = Hq // Hkv
        q = (lambda x, *d: x.permute(*d))((x @ wq).reshape(B, Sq, Hq, Dh), 0, 2, 1, 3)
        k = (lambda x, n, axis: torch.repeat_interleave(x, n, dim=axis))(k_cache[:, :, Sk - W:, :], rep, axis=1)
        v = (lambda x, n, axis: torch.repeat_interleave(x, n, dim=axis))(v_cache[:, :, Sk - W:, :], rep, axis=1)
        scores = torch.einsum("bhqd,bhkd->bhqk", q, k) * (Dh ** -0.5)
        p = _softmax(scores, axis=-1)
        o = torch.einsum("bhqk,bhkd->bhqd", p, v)
        return (lambda x, *d: x.permute(*d))(o, 0, 2, 1, 3).reshape(B, Sq, Hq * Dh) @ wo

    def _B_ref(x, k_cache, v_cache, wq, wo):
        B, Sq, D = x.shape
        Hq, Dh = 32, 128
        Hkv, Sk = k_cache.shape[1], k_cache.shape[2]
        W = min(4096, max(256, Sk // 4))
        rep = Hq // Hkv
        q = (lambda x, *d: x.permute(*d))((x @ wq).reshape(B, Sq, Hq, Dh), 0, 2, 1, 3)
        k = (lambda x, n, axis: torch.repeat_interleave(x, n, dim=axis))(k_cache[:, :, Sk - W:, :], rep, axis=1)
        v = (lambda x, n, axis: torch.repeat_interleave(x, n, dim=axis))(v_cache[:, :, Sk - W:, :], rep, axis=1)
        scores = torch.einsum("bhqd,bhkd->bhqk", q, k) * (Dh ** -0.5)
        p = _softmax(scores, axis=-1)
        o = torch.einsum("bhqk,bhkd->bhqd", p, v)
        return (lambda x, *d: x.permute(*d))(o, 0, 2, 1, 3).reshape(B, Sq, Hq * Dh) @ wo

    a = _A_ref(A_x, A_k_cache, A_v_cache, A_wq, A_wo)
    b = _B_ref(B_x, B_k_cache, B_v_cache, B_wq, B_wo)
    return torch.concatenate([a, b], axis=-1)
```

```cpp
#include <hip/hip_runtime.h>
#include <hip/hip_bf16.h>
#include <stdint.h>

#define GAS __attribute__((address_space(1)))
#define LAS __attribute__((address_space(3)))
typedef unsigned v4u __attribute__((ext_vector_type(4)));
typedef unsigned v2u __attribute__((ext_vector_type(2)));
typedef float f32x4 __attribute__((ext_vector_type(4)));
typedef float f32x2 __attribute__((ext_vector_type(2)));
typedef __bf16 bf16x8 __attribute__((ext_vector_type(8)));
typedef __bf16 bf16x2 __attribute__((ext_vector_type(2)));
typedef unsigned long long u64;

namespace {

constexpr int NB = 8, DM = 512, HQ = 32, HKV = 8, SK = 4096, DH = 128, WIN = 1024;
constexpr int NWG = 256, NTHR = 768;
constexpr unsigned SENT = 0xFFFFFFFFu;
constexpr int WS_EPOCH = 0;
constexpr int WS_FLAGS = 4096;
constexpr int WS_Q = 8192;
constexpr int WS_O = WS_Q + 16 * 8 * 1024;
constexpr int WS_ML = WS_O + 16 * 16 * 2048;
constexpr int WS_P = WS_ML + 16 * 4 * 128;
constexpr int WS_END = WS_P + 256 * 4096;
constexpr int WS_STAMP = (WS_END + 4095) / 4096 * 4096;
constexpr unsigned SPIN_CAP = 1u << 16;

constexpr int L_QPART = 0;
constexpr int RB = 528;
constexpr int L_RED2 = 0;
constexpr int L_OPART = 4 * 8 * RB;
constexpr int L_MLW = L_OPART + 16384;
constexpr int L_QF = L_MLW + 256;
constexpr int L_SYNC = L_QF + 1024;
constexpr int L_BYTES = L_SYNC + 64;
constexpr int S_QP = 0, S_QRDY = 1, S_KISS = 2, S_VISS = 3, S_MLC = 4, S_TAGV = 5, S_FAIL = 6, S_TAG = 8;

struct Params {
  const unsigned short* x[2];
  const unsigned short* k[2];
  const unsigned short* v[2];
  const unsigned short* wq[2];
  const unsigned short* wo[2];
  unsigned short* out;
  unsigned char* ws;
};

__device__ __forceinline__ f32x4 mfma16(v4u a, v4u b, f32x4 c) {
  return __builtin_amdgcn_mfma_f32_16x16x32_bf16(__builtin_bit_cast(bf16x8, a), __builtin_bit_cast(bf16x8, b), c, 0, 0, 0);
}
__device__ __forceinline__ v4u ldnt(const unsigned short* p) { return __builtin_nontemporal_load((const GAS v4u*)p); }
__device__ __forceinline__ unsigned cvtpk(float lo, float hi) {
  f32x2 f = {lo, hi};
  bf16x2 r = __builtin_convertvector(f, bf16x2);
  return __builtin_bit_cast(unsigned, r);
}
__device__ __forceinline__ unsigned nosent(unsigned u) { return u == SENT ? 0x7FC07FC0u : u; }
template <int CTRL> __device__ __forceinline__ float dppf(float x) {
  return __builtin_bit_cast(float, __builtin_amdgcn_mov_dpp(__builtin_bit_cast(int, x), CTRL, 0xf, 0xf, true));
}
template <int CTRL> __device__ __forceinline__ unsigned dppu(unsigned x) {
  return (unsigned)__builtin_amdgcn_mov_dpp((int)x, CTRL, 0xf, 0xf, true);
}
template <int BANK> __device__ __forceinline__ float selbank(float old, float x) {
  return __builtin_bit_cast(float, __builtin_amdgcn_update_dpp(__builtin_bit_cast(int, old), __builtin_bit_cast(int, x), 0xE4, 0xf, 1 << BANK, false));
}
constexpr int ROR4 = 0x124, ROR8 = 0x128, ROR12 = 0x12C, SHL4 = 0x104, SHL8 = 0x108;
__device__ __forceinline__ float xrow_max(float x) {
  auto s = __builtin_amdgcn_permlane16_swap(__float_as_uint(x), __float_as_uint(x), false, false);
  x = fmaxf(__uint_as_float(s[0]), __uint_as_float(s[1]));
  auto t = __builtin_amdgcn_permlane32_swap(__float_as_uint(x), __float_as_uint(x), false, false);
  return fmaxf(__uint_as_float(t[0]), __uint_as_float(t[1]));
}
__device__ __forceinline__ float xrow_sum(float x) {
  auto s = __builtin_amdgcn_permlane16_swap(__float_as_uint(x), __float_as_uint(x), false, false);
  x = __uint_as_float(s[0]) + __uint_as_float(s[1]);
  auto t = __builtin_amdgcn_permlane32_swap(__float_as_uint(x), __float_as_uint(x), false, false);
  return __uint_as_float(t[0]) + __uint_as_float(t[1]);
}
#define BAR() asm volatile("s_waitcnt lgkmcnt(0)\n\ts_barrier" ::: "memory")
#define SC1 16
__device__ __forceinline__ v4u ld_sc1(__amdgpu_buffer_rsrc_t rs, int off) { return __builtin_amdgcn_raw_buffer_load_b128(rs, off, 0, SC1); }
__device__ __forceinline__ v2u ld_sc1_8(__amdgpu_buffer_rsrc_t rs, int off) { return __builtin_amdgcn_raw_buffer_load_b64(rs, off, 0, SC1); }
__device__ __forceinline__ void st_sc1(__amdgpu_buffer_rsrc_t rs, int off, v4u v) { __builtin_amdgcn_raw_buffer_store_b128(v, rs, off, 0, SC1); }
__device__ __forceinline__ void st_sc1_8(__amdgpu_buffer_rsrc_t rs, int off, v2u v) { __builtin_amdgcn_raw_buffer_store_b64(v, rs, off, 0, SC1); }
__device__ __forceinline__ void st_sc1_4(__amdgpu_buffer_rsrc_t rs, int off, unsigned v) { __builtin_amdgcn_raw_buffer_store_b32(v, rs, off, 0, SC1); }
__device__ __forceinline__ bool valid4(v4u a) { return a.x != SENT && a.y != SENT && a.z != SENT && a.w != SENT; }

template <int D> __device__ __forceinline__ v4u colfrag(const v4u* r) {
  constexpr unsigned sel = (D & 1) ? 0x07060302u : 0x05040100u;
  v4u o;
  o.x = __builtin_amdgcn_perm(r[1][D >> 1], r[0][D >> 1], sel);
  o.y = __builtin_amdgcn_perm(r[3][D >> 1], r[2][D >> 1], sel);
  o.z = __builtin_amdgcn_perm(r[5][D >> 1], r[4][D >> 1], sel);
  o.w = __builtin_amdgcn_perm(r[7][D >> 1], r[6][D >> 1], sel);
  return o;
}

__device__ __forceinline__ void lds_wait(LAS unsigned* w, unsigned target) {
  asm volatile("" ::: "memory");
  while ((unsigned)__builtin_amdgcn_readfirstlane(__hip_atomic_load(w, __ATOMIC_RELAXED, __HIP_MEMORY_SCOPE_WORKGROUP)) < target)
    __builtin_amdgcn_s_sleep(1);
  asm volatile("" ::: "memory");
}
__device__ __forceinline__ void lds_signal(LAS unsigned* w, int lane) {
  asm volatile("" ::: "memory");
  if (lane == 0) __hip_atomic_fetch_add(w, 1u, __ATOMIC_RELAXED, __HIP_MEMORY_SCOPE_WORKGROUP);
  asm volatile("" ::: "memory");
}
template <int Q> __device__ __forceinline__ void pairfrag(const v4u& r0, const v4u& r1, v4u* bf) {
#pragma unroll
  for (int d = 0; d < 8; ++d) bf[d][Q] = __builtin_amdgcn_perm(r1[d >> 1], r0[d >> 1], (d & 1) ? 0x07060302u : 0x05040100u);
}

#define STAMP(i) do {} while (0)
#define FLUSH() do {} while (0)
#define FLAGWAIT 2
#define DEPTH 4
#define THR() asm volatile("s_waitcnt vmcnt(%0)" :: "n"(DEPTH - 1) : "memory")
#define NT 2
#define SB() __builtin_amdgcn_sched_barrier(0)

__device__ __forceinline__ void wg_stats(const unsigned char* lds, int lane, float& M, float& L) {
  const LAS f32x4* mlp = (const LAS f32x4*)(lds + L_MLW + (lane & 3) * 64);
  const f32x4 t0 = mlp[0], t1 = mlp[1], t2 = mlp[2], t3 = mlp[3];
  M = fmaxf(fmaxf(fmaxf(t0.x, t0.z), fmaxf(t1.x, t1.z)), fmaxf(fmaxf(t2.x, t2.z), fmaxf(t3.x, t3.z)));
  L = (t0.y * __builtin_amdgcn_exp2f(t0.x - M) + t0.w * __builtin_amdgcn_exp2f(t0.z - M)) +
      (t1.y * __builtin_amdgcn_exp2f(t1.x - M) + t1.w * __builtin_amdgcn_exp2f(t1.z - M)) +
      ((t2.y * __builtin_amdgcn_exp2f(t2.x - M) + t2.w * __builtin_amdgcn_exp2f(t2.z - M)) +
       (t3.y * __builtin_amdgcn_exp2f(t3.x - M) + t3.w * __builtin_amdgcn_exp2f(t3.z - M)));
}

__global__ void __launch_bounds__(NTHR, 3) fused_decode(Params p) {
  __shared__ __attribute__((aligned(16))) unsigned char lds[L_BYTES];
  const int tid = threadIdx.x, lane = tid & 63;
  const int wave = __builtin_amdgcn_readfirstlane(tid >> 6);
  const int id = blockIdx.x;
  const int kvh = id & 7, br = id >> 7, j = (id >> 3) & 15;
  const int grp = br * 8 + kvh;
  const int lg = lane >> 4, c16 = lane & 15;
  const __amdgpu_buffer_rsrc_t rs = __builtin_amdgcn_make_buffer_rsrc(p.ws, 0, WS_END, 0x00020000);
  LAS unsigned* sync = (LAS unsigned*)(lds + L_SYNC);
  STAMP(0);
  if (wave < 4) {
    const int pw = wave;
    __builtin_amdgcn_s_setprio(2);
    bool fail = false;
    if (pw < 2) {
      const int rl = c16 >> 2, cl = lane & 3;
      const unsigned short* wqp = p.wq[br] + (size_t)(256 * pw + 32 * rl + 8 * lg) * (HQ * DH) + kvh * 512 + j * 32 + cl * 8;
      v4u w[16];
#pragma unroll
      for (int i = 0; i < 16; ++i) w[i] = ldnt(wqp + (size_t)((i & 7) + (i >> 3) * 128) * (HQ * DH));
      const unsigned short* xp = p.x[br] + (lane & 3) * DM + 256 * pw + 32 * rl + 8 * lg;
      const v4u xa0 = *(const GAS v4u*)xp, xa1 = *(const GAS v4u*)(xp + 4 * DM);
      const v4u xb0 = *(const GAS v4u*)(xp + 128), xb1 = *(const GAS v4u*)(xp + 4 * DM + 128);
      if (pw == 0 && lane < 8) sync[lane] = 0u;
      SB();
      BAR();
      SB();
      f32x4 a0[8], a1[8];
#define QSTEP(D) { const v4u bfr = colfrag<D>(w); a0[D] = mfma16(xa0, bfr, f32x4{0.f, 0.f, 0.f, 0.f}); a1[D] = mfma16(xa1, bfr, f32x4{0.f, 0.f, 0.f, 0.f}); }
      QSTEP(0) QSTEP(1) QSTEP(2) QSTEP(3) QSTEP(4) QSTEP(5) QSTEP(6) QSTEP(7)
#undef QSTEP
#define QSTEP(D) { const v4u bfr = colfrag<D>(w + 8); a0[D] = mfma16(xb0, bfr, a0[D]); a1[D] = mfma16(xb1, bfr, a1[D]); }
      QSTEP(0) QSTEP(1) QSTEP(2) QSTEP(3) QSTEP(4) QSTEP(5) QSTEP(6) QSTEP(7)
#undef QSTEP
      STAMP(1);
      if (rl == lg) {
        LAS float* qp = (LAS float*)(lds + L_QPART) + (pw * 4 + lg) * 256 + cl * 8;
#pragma unroll
        for (int v = 0; v < 4; ++v) {
          *(LAS f32x4*)(qp + v * 32) = f32x4{a0[0][v], a0[1][v], a0[2][v], a0[3][v]};
          *(LAS f32x4*)(qp + v * 32 + 4) = f32x4{a0[4][v], a0[5][v], a0[6][v], a0[7][v]};
          *(LAS f32x4*)(qp + (4 + v) * 32) = f32x4{a1[0][v], a1[1][v], a1[2][v], a1[3][v]};
          *(LAS f32x4*)(qp + (4 + v) * 32 + 4) = f32x4{a1[4][v], a1[5][v], a1[6][v], a1[7][v]};
        }
      }
      lds_signal(sync + S_QP, lane);
      lds_wait(sync + S_QP, 2);
      const LAS f32x2* qp = (const LAS f32x2*)(lds + L_QPART) + tid;
      f32x2 s = {0.f, 0.f};
#pragma unroll
      for (int i = 0; i < 8; ++i) s += qp[i * 128];
      const unsigned u = nosent(cvtpk(s.x, s.y));
      const v4u q4 = {dppu<0x00>(u), dppu<0x55>(u), dppu<0xAA>(u), dppu<0xFF>(u)};
      lds_wait(sync + S_TAGV, 1);
      if ((lane & 3) == 0) st_sc1(rs, WS_Q + (grp * 8 + (tid >> 4)) * 1024 + j * 64 + (tid & 12) * 4, q4);
      lds_signal(sync + S_QP, lane);
      STAMP(2);
      if (pw == 1) {
        lds_wait(sync + S_MLC, 8);
        float M, L;
        wg_stats(lds, lane, M, L);
        if (lane < 4) st_sc1_8(rs, WS_ML + ((grp * 4 + lane) * 16 + j) * 8, v2u{nosent(__float_as_uint(M)), nosent(__float_as_uint(L))});
        STAMP(3);
      }
    } else if (pw == 2) {
      BAR();
      lds_wait(sync + S_QP, FLAGWAIT);
      lds_wait(sync + S_TAGV, 1);
      const v2u tagv = *(LAS v2u*)(sync + S_TAG);
      STAMP(1);
      for (unsigned spins = 0;; ++spins) {
        bool ok = true;
        if (lane < 8) {
          const v4u f = ld_sc1(rs, WS_FLAGS + grp * 128 + lane * 16);
          ok = f.x == tagv.x && f.y == tagv.y && f.z == tagv.x && f.w == tagv.y;
        }
        if (__all(ok)) break;
        if (spins > SPIN_CAP) { fail = true; break; }
        asm volatile("s_sleep 1" ::: "memory");
      }
      STAMP(2);
      asm volatile("" ::: "memory");
      v4u qf;
      const int qoff = WS_Q + (grp * 8 + (j >> 1)) * 1024 + (lane & 3) * 256 + (lg * 4 + (c16 >> 2)) * 16;
      for (unsigned spins = 0;; ++spins) {
        qf = ld_sc1(rs, qoff);
        if (__all(valid4(qf))) break;
        if (spins > SPIN_CAP) { fail = true; break; }
        asm volatile("s_sleep 1" ::: "memory");
      }
      *(LAS v4u*)(lds + L_QF + lane * 16) = qf;
      lds_signal(sync + S_QRDY, lane);
      STAMP(3);
    } else {
      const v2u ev = ld_sc1_8(rs, WS_EPOCH);
      const v4u s4 = {SENT, SENT, SENT, SENT};
      const int wg = grp * 16 + j;
      if (lane < 32) st_sc1(rs, WS_Q + (grp * 8 + (lane >> 2)) * 1024 + j * 64 + (lane & 3) * 16, s4);
      else if (lane < 36) st_sc1_8(rs, WS_ML + ((grp * 4 + (lane - 32)) * 16 + j) * 8, v2u{SENT, SENT});
      st_sc1(rs, WS_O + wg * 2048 + lane * 16, s4);
      st_sc1(rs, WS_O + wg * 2048 + 1024 + lane * 16, s4);
#pragma unroll
      for (int i = 0; i < 4; ++i) st_sc1(rs, WS_P + wg * 4096 + i * 1024 + lane * 16, s4);
      SB();
      BAR();
      asm volatile("s_waitcnt vmcnt(0)" ::: "memory");
      const u64 t = (((u64)ev.y << 32) | ev.x) + 1ull;
      const v2u tagv = {(unsigned)t, (unsigned)(t >> 32)};
      if (lane == 0) {
        st_sc1_8(rs, WS_FLAGS + (grp * 16 + j) * 8, tagv);
        *(LAS v2u*)(sync + S_TAG) = tagv;
      }
      lds_signal(sync + S_TAGV, lane);
      STAMP(1);
      lds_wait(sync + S_MLC, 8);
      for (unsigned spins = 0;; ++spins) {
        if (spins) asm volatile("s_sleep 8" ::: "memory");
        const v4u f0 = ld_sc1(rs, WS_FLAGS + br * 1024 + lane * 16);
        bool ok = f0.x == tagv.x && f0.y == tagv.y && f0.z == tagv.x && f0.w == tagv.y;
        if (id == 0) {
          const v4u f1 = ld_sc1(rs, WS_FLAGS + 1024 + lane * 16);
          ok = ok && f1.x == tagv.x && f1.y == tagv.y && f1.z == tagv.x && f1.w == tagv.y;
        }
        if (__all(ok)) break;
        if (spins > SPIN_CAP) { fail = true; break; }
      }
      if (id == 0 && lane == 0) st_sc1_8(rs, WS_EPOCH, tagv);
      STAMP(2);
    }
    const int hh = j >> 2, cq = j & 3;
    const unsigned short* wop = p.wo[br] + (size_t)((kvh * 4 + hh) * 128 + 32 * pw + 8 * lg) * DM + cq * 128 + c16 * 8;
    lds_wait(sync + S_VISS, 8);
    STAMP(4);
    v4u w[8];
#pragma unroll
    for (int i = 0; i < 8; ++i) w[i] = ldnt(wop + (size_t)i * DM);
    SB();
    BAR();
    SB();
    STAMP(5);
    const int jp = 2 * (lane & 7) + ((lane >> 3) & 1);
    float wgt;
    {
      v2u ml;
      const int mloff = WS_ML + ((grp * 4 + hh) * 16 + jp) * 8;
      for (unsigned spins = 0;; ++spins) {
        ml = ld_sc1_8(rs, mloff);
        if (__all(ml.x != SENT && ml.y != SENT)) break;
        if (spins > SPIN_CAP) { fail = true; break; }
        asm volatile("s_sleep 1" ::: "memory");
      }
      const float m = __uint_as_float(ml.x), l = __uint_as_float(ml.y);
      const float m2 = dppf<ROR8>(m), l2 = dppf<ROR8>(l);
      const float M = fmaxf(m, m2);
      const float a = l * __builtin_amdgcn_exp2f(m - M), a2 = l2 * __builtin_amdgcn_exp2f(m2 - M);
      wgt = a / (a + a2);
    }
    v4u af[8];
    pairfrag<0>(w[0], w[1], af); pairfrag<1>(w[2], w[3], af); pairfrag<2>(w[4], w[5], af); pairfrag<3>(w[6], w[7], af);
#pragma unroll
    for (int i = 0; i < 8; ++i) asm volatile("" : "+v"(af[i]));
    asm volatile("" : "+v"(wgt));
    STAMP(6);
    {
      v4u o0, o1;
      const int ooff = WS_O + ((grp * 16 + jp) * 4 + hh) * 512 + (32 * pw + 8 * lg) * 4;
      for (unsigned spins = 0;; ++spins) {
        o0 = ld_sc1(rs, ooff);
        o1 = ld_sc1(rs, ooff + 16);
        if (__all(valid4(o0) && valid4(o1))) break;
        if (spins > SPIN_CAP) { fail = true; break; }
        asm volatile("s_sleep 1" ::: "memory");
      }
      STAMP(7);
      f32x4 f0 = __builtin_bit_cast(f32x4, o0) * wgt, f1 = __builtin_bit_cast(f32x4, o1) * wgt;
#pragma unroll
      for (int e = 0; e < 4; ++e) { f0[e] += dppf<ROR8>(f0[e]); f1[e] += dppf<ROR8>(f1[e]); }
      const v4u ob = {cvtpk(f0[0], f0[1]), cvtpk(f0[2], f0[3]), cvtpk(f1[0], f1[1]), cvtpk(f1[2], f1[3])};
      if (fail && lane == 0) sync[S_FAIL] = 1u;
      f32x4 acc[8];
#pragma unroll
      for (int d = 0; d < 8; ++d) acc[d] = mfma16(af[d], ob, f32x4{0.f, 0.f, 0.f, 0.f});
      if (c16 < 8) {
        LAS unsigned char* r2 = (LAS unsigned char*)lds + L_RED2 + (pw * 8 + c16) * RB + lg * 128;
#pragma unroll
        for (int r = 0; r < 4; ++r) {
          *(LAS f32x4*)(r2 + r * 32) = f32x4{acc[0][r], acc[1][r], acc[2][r], acc[3][r]};
          *(LAS f32x4*)(r2 + r * 32 + 16) = f32x4{acc[4][r], acc[5][r], acc[6][r], acc[7][r]};
        }
      }
    }
    BAR();
    {
      const LAS unsigned char* r2 = (const LAS unsigned char*)lds + L_RED2 + (tid >> 5) * RB + (tid & 31) * 16;
      const f32x4 o = (*(const LAS f32x4*)r2 + *(const LAS f32x4*)(r2 + 8 * RB)) + (*(const LAS f32x4*)(r2 + 16 * RB) + *(const LAS f32x4*)(r2 + 24 * RB));
      v4u ou = __builtin_bit_cast(v4u, o);
      ou.x = nosent(ou.x); ou.y = nosent(ou.y); ou.z = nosent(ou.z); ou.w = nosent(ou.w);
      st_sc1(rs, WS_P + (grp * 16 + j) * 4096 + tid * 16, ou);
      STAMP(8);
    }
    if (pw == 0) {
      const int r = kvh * 16 + j, cq2 = r >> 5, bo = (r >> 2) & 7, c32 = r & 3;
      fail = sync[S_FAIL] != 0u;
      v4u pv[4];
      const int poff = WS_P + ((br * 8 + (lane >> 3)) * 16 + cq2) * 4096 + bo * 512 + c32 * 128 + (lane & 7) * 16;
      for (unsigned spins = 0;; ++spins) {
        bool ok = true;
#pragma unroll
        for (int it = 0; it < 4; ++it) { pv[it] = ld_sc1(rs, poff + it * 4 * 4096); ok = ok && valid4(pv[it]); }
        if (__all(ok)) break;
        if (spins > SPIN_CAP) { fail = true; break; }
        asm volatile("s_sleep 1" ::: "memory");
      }
      STAMP(9);
      f32x4 t = (__builtin_bit_cast(f32x4, pv[0]) + __builtin_bit_cast(f32x4, pv[1])) + (__builtin_bit_cast(f32x4, pv[2]) + __builtin_bit_cast(f32x4, pv[3]));
#pragma unroll
      for (int e = 0; e < 4; ++e) { float x = t[e]; x += dppf<ROR8>(x); t[e] = xrow_sum(x); }
      if (fail) t = f32x4{__builtin_nanf(""), __builtin_nanf(""), __builtin_nanf(""), __builtin_nanf("")};
      if (lane < 8) {
        v2u o = {cvtpk(t[0], t[1]), cvtpk(t[2], t[3])};
        *(GAS v2u*)(p.out + bo * 1024 + br * 512 + cq2 * 128 + c32 * 32 + lane * 4) = o;
      }
      STAMP(10);
    }
    FLUSH();
  } else {
    const int w = wave - 4;
    const int b = j >> 1, s = j & 1;
    const size_t base = ((size_t)(b * HKV + kvh) * SK + (SK - WIN) + s * 512 + w * 64) * DH;
    const __amdgpu_buffer_rsrc_t rk = __builtin_amdgcn_make_buffer_rsrc((void*)(p.k[br] + base), 0, 16384, 0x00020000);
    const __amdgpu_buffer_rsrc_t rv = __builtin_amdgcn_make_buffer_rsrc((void*)(p.v[br] + base), 0, 16384, 0x00020000);
    const int ko = (c16 >> 2) * 256 + lg * 64 + (lane & 3) * 16, vo = lane * 16;
    const int rot = (w * 2 + j * 5 + kvh * 3 + br * 8) & 15;
#define RI(i) ((((i) + rot) & 15) << 10)
    BAR();
    SB();
    v4u kr[16], vr[16];
#pragma unroll
    for (int i = 0; i < 16; ++i) {
      if (i >= DEPTH) THR();
      kr[i] = __builtin_amdgcn_raw_buffer_load_b128(rk, ko, RI(i), NT);
      SB();
    }
    STAMP(1);
#define VLD(i) do { THR(); vr[i] = __builtin_amdgcn_raw_buffer_load_b128(rv, vo, RI(i), NT); SB(); } while (0)
    VLD(0); VLD(1); VLD(2); VLD(3);
    STAMP(2);
    lds_wait(sync + S_QRDY, 1);
    STAMP(3);
    const v4u qf = *(const LAS v4u*)(lds + L_QF + lane * 16);
    float R[4];
#define QK(rr) do { \
      f32x4 Dq[4]; \
      _Pragma("unroll") for (int t = 0; t < 4; ++t) Dq[t] = mfma16(kr[rr * 4 + t], qf, f32x4{0.f, 0.f, 0.f, 0.f}); \
      float U[4]; \
      _Pragma("unroll") for (int rho = 0; rho < 4; ++rho) { \
        float u = Dq[rho][0]; \
        u = selbank<1>(u, Dq[(rho + 1) & 3][1]); \
        u = selbank<2>(u, Dq[(rho + 2) & 3][2]); \
        u = selbank<3>(u, Dq[(rho + 3) & 3][3]); \
        U[rho] = u; \
      } \
      R[rr] = (U[0] + dppf<ROR4>(U[1])) + (dppf<ROR8>(U[2]) + dppf<ROR12>(U[3])); \
      asm volatile("" : "+v"(R[rr])); \
      SB(); } while (0)
    QK(0); QK(1);
    VLD(4);
    QK(2); QK(3);
    VLD(5);
    constexpr float C2 = 0.088388347648318440f * 1.4426950408889634f;
    {
      float m = fmaxf(fmaxf(R[0], R[1]), fmaxf(R[2], R[3]));
      m = fmaxf(m, dppf<ROR4>(m));
      m = fmaxf(m, dppf<ROR8>(m));
      m = xrow_max(m) * C2;
      float l = 0.f;
#pragma unroll
      for (int rr = 0; rr < 4; ++rr) l += __builtin_amdgcn_exp2f(fmaf(R[rr], C2, -m));
      l += dppf<ROR4>(l);
      l += dppf<ROR8>(l);
      l = xrow_sum(l);
      if (lane < 4) *(LAS f32x2*)(lds + L_MLW + (lane * 8 + w) * 8) = f32x2{m, l};
      lds_signal(sync + S_MLC, lane);
    }
    STAMP(4);
    SB();
    v4u b0[8], b1[8];
    VLD(6); pairfrag<0>(vr[0], vr[1], b0); SB();
    VLD(7);
    VLD(8); pairfrag<1>(vr[2], vr[3], b0); SB();
    VLD(9);
    lds_wait(sync + S_MLC, 8);
    v4u pa[2];
    {
      float M, L;
      wg_stats(lds, lane, M, L);
      const float inv = 1.0f / L;
#pragma unroll
      for (int rr = 0; rr < 4; ++rr) {
        const float pr = __builtin_amdgcn_exp2f(fmaf(R[rr], C2, -M)) * inv;
        const unsigned x = cvtpk(pr, dppf<SHL4>(pr));
        pa[rr >> 1][(rr & 1) * 2] = x;
        pa[rr >> 1][(rr & 1) * 2 + 1] = dppu<SHL8>(x);
      }
      asm volatile("" : "+v"(pa[0]), "+v"(pa[1]));
    }
    STAMP(5);
    SB();
    VLD(10); pairfrag<2>(vr[4], vr[5], b0); SB();
    VLD(11);
    VLD(12); pairfrag<3>(vr[6], vr[7], b0);
    f32x4 acc[8];
#pragma unroll
    for (int d = 0; d < 8; ++d) acc[d] = mfma16(pa[0], b0[d], f32x4{0.f, 0.f, 0.f, 0.f});
    SB();
    VLD(13);
    VLD(14); pairfrag<0>(vr[8], vr[9], b1); SB();
    VLD(15);
    lds_signal(sync + S_VISS, lane);
    STAMP(6);
    SB();
    pairfrag<1>(vr[10], vr[11], b1); SB();
    pairfrag<2>(vr[12], vr[13], b1); SB();
    pairfrag<3>(vr[14], vr[15], b1);
#pragma unroll
    for (int d = 0; d < 8; ++d) acc[d] = mfma16(pa[1], b1[d], acc[d]);
    if (lane < 16) {
      LAS float* op = (LAS float*)(lds + L_OPART) + w * 512 + lane * 4;
#pragma unroll
      for (int h = 0; h < 4; ++h) {
        *(LAS f32x4*)(op + h * 128) = f32x4{acc[0][h], acc[1][h], acc[2][h], acc[3][h]};
        *(LAS f32x4*)(op + h * 128 + 64) = f32x4{acc[4][h], acc[5][h], acc[6][h], acc[7][h]};
      }
    }
    STAMP(7);
    BAR();
    if (w < 2) {
      const int e = w * 64 + lane;
      const LAS f32x4* op = (const LAS f32x4*)(lds + L_OPART) + e;
      const f32x4 o = ((op[0] + op[128]) + (op[256] + op[384])) + ((op[512] + op[640]) + (op[768] + op[896]));
      v4u ou = __builtin_bit_cast(v4u, o);
      ou.x = nosent(ou.x); ou.y = nosent(ou.y); ou.z = nosent(ou.z); ou.w = nosent(ou.w);
      st_sc1(rs, WS_O + (grp * 16 + j) * 2048 + (e >> 5) * 512 + (e & 15) * 32 + ((e >> 4) & 1) * 16, ou);
      STAMP(8);
    }
    FLUSH();
  }
}

}

extern "C" void kernel_launch(void* const* d_in, const int* in_sizes, int n_in, void* d_out, int out_size, void* d_ws, size_t ws_size,
                              hipStream_t stream) {
  Params p;
  for (int br = 0; br < 2; ++br) {
    p.x[br] = (const unsigned short*)d_in[br * 5 + 0];
    p.k[br] = (const unsigned short*)d_in[br * 5 + 1];
    p.v[br] = (const unsigned short*)d_in[br * 5 + 2];
    p.wq[br] = (const unsigned short*)d_in[br * 5 + 3];
    p.wo[br] = (const unsigned short*)d_in[br * 5 + 4];
  }
  p.out = (unsigned short*)d_out;
  p.ws = (unsigned char*)d_ws;
  fused_decode<<<NWG, NTHR, 0, stream>>>(p);
}
```

```cpp
#include <hip/hip_runtime.h>
#include <hip/hip_bf16.h>
#include <stdint.h>

#define GAS __attribute__((address_space(1)))
#define LAS __attribute__((address_space(3)))
typedef unsigned v4u __attribute__((ext_vector_type(4)));
typedef unsigned v2u __attribute__((ext_vector_type(2)));
typedef float f32x4 __attribute__((ext_vector_type(4)));
typedef float f32x2 __attribute__((ext_vector_type(2)));
typedef __bf16 bf16x8 __attribute__((ext_vector_type(8)));
typedef __bf16 bf16x2 __attribute__((ext_vector_type(2)));
typedef unsigned long long u64;

namespace {

constexpr int NB = 8, DM = 512, HQ = 32, HKV = 8, SK = 4096, DH = 128, WIN = 1024;
constexpr int NWG = 256, NTHR = 768;
constexpr unsigned SENT = 0xFFFFFFFFu;
constexpr int WS_EPOCH = 0;
constexpr int WS_FLAGS = 4096;
constexpr int WS_ML = WS_FLAGS + 128;
constexpr int WS_BOX = WS_FLAGS + 256 * 256;
constexpr int BOX = 7168;
constexpr int WS_Q = WS_BOX;
constexpr int WS_O = WS_BOX + 2048;
constexpr int WS_P = WS_BOX + 3072;
constexpr int WS_END = WS_BOX + 256 * BOX;
constexpr unsigned SPIN_CAP = 1u << 16;

constexpr int L_QPART = 0;
constexpr int L_OPART = 8192;
constexpr int L_MLW = L_OPART + 16384;
constexpr int L_QF = L_MLW + 256;
constexpr int L_SYNC = L_QF + 1024;
constexpr int L_BYTES = L_SYNC + 128;
constexpr int S_QP = 0, S_QRDY = 1, S_VISS = 3, S_MLC = 4, S_TAGV = 5, S_FAIL = 6, S_PPUB = 7, S_TAG = 16, S_WV = 20;
constexpr int OOB = 0x7FFFFF00;

struct Params {
  const unsigned short* in[10];
  unsigned short* out;
  unsigned char* ws;
};

__device__ __forceinline__ f32x4 mfma16(v4u a, v4u b, f32x4 c) {
  return __builtin_amdgcn_mfma_f32_16x16x32_bf16(__builtin_bit_cast(bf16x8, a), __builtin_bit_cast(bf16x8, b), c, 0, 0, 0);
}
__device__ __forceinline__ unsigned cvtpk(float lo, float hi) {
  f32x2 f = {lo, hi};
  bf16x2 r = __builtin_convertvector(f, bf16x2);
  return __builtin_bit_cast(unsigned, r);
}
__device__ __forceinline__ unsigned nosent(unsigned u) { return u == SENT ? 0x7FC07FC0u : u; }
template <int CTRL> __device__ __forceinline__ float dppf(float x) {
  return __builtin_bit_cast(float, __builtin_amdgcn_mov_dpp(__builtin_bit_cast(int, x), CTRL, 0xf, 0xf, true));
}
template <int CTRL> __device__ __forceinline__ unsigned dppu(unsigned x) {
  return (unsigned)__builtin_amdgcn_mov_dpp((int)x, CTRL, 0xf, 0xf, true);
}
template <int BANK> __device__ __forceinline__ float selbank(float old, float x) {
  return __builtin_bit_cast(float, __builtin_amdgcn_update_dpp(__builtin_bit_cast(int, old), __builtin_bit_cast(int, x), 0xE4, 0xf, 1 << BANK, false));
}
constexpr int ROR4 = 0x124, ROR8 = 0x128, ROR12 = 0x12C, SHL4 = 0x104, SHL8 = 0x108;
__device__ __forceinline__ float xrow_max(float x) {
  auto s = __builtin_amdgcn_permlane16_swap(__float_as_uint(x), __float_as_uint(x), false, false);
  x = fmaxf(__uint_as_float(s[0]), __uint_as_float(s[1]));
  auto t = __builtin_amdgcn_permlane32_swap(__float_as_uint(x), __float_as_uint(x), false, false);
  return fmaxf(__uint_as_float(t[0]), __uint_as_float(t[1]));
}
__device__ __forceinline__ float xrow_sum(float x) {
  auto s = __builtin_amdgcn_permlane16_swap(__float_as_uint(x), __float_as_uint(x), false, false);
  x = __uint_as_float(s[0]) + __uint_as_float(s[1]);
  auto t = __builtin_amdgcn_permlane32_swap(__float_as_uint(x), __float_as_uint(x), false, false);
  return __uint_as_float(t[0]) + __uint_as_float(t[1]);
}
#define BAR() asm volatile("s_waitcnt lgkmcnt(0)\n\ts_barrier" ::: "memory")
#define SC1 16
__device__ __forceinline__ v2u ld_sc1_8(__amdgpu_buffer_rsrc_t rs, int off) { return __builtin_amdgcn_raw_buffer_load_b64(rs, off, 0, SC1); }
__device__ __forceinline__ void st_sc1(__amdgpu_buffer_rsrc_t rs, int off, v4u v) { __builtin_amdgcn_raw_buffer_store_b128(v, rs, off, 0, SC1); }
__device__ __forceinline__ void st_sc1_8(__amdgpu_buffer_rsrc_t rs, int off, v2u v) { __builtin_amdgcn_raw_buffer_store_b64(v, rs, off, 0, SC1); }

template <int D> __device__ __forceinline__ v4u colfrag(const v4u* r) {
  constexpr unsigned sel = (D & 1) ? 0x07060302u : 0x05040100u;
  v4u o;
  o.x = __builtin_amdgcn_perm(r[1][D >> 1], r[0][D >> 1], sel);
  o.y = __builtin_amdgcn_perm(r[3][D >> 1], r[2][D >> 1], sel);
  o.z = __builtin_amdgcn_perm(r[5][D >> 1], r[4][D >> 1], sel);
  o.w = __builtin_amdgcn_perm(r[7][D >> 1], r[6][D >> 1], sel);
  return o;
}


template <int TARGET> __device__ __forceinline__ void lds_wait(LAS unsigned* w) {
  unsigned v, c, n = 1u << 24;
  asm volatile("s_branch 2f\n"
               "1:\n\ts_sleep 1\n\t"
               "s_sub_u32 %[n], %[n], 1\n\t"
               "s_cbranch_scc1 3f\n"
               "2:\n\tds_read_b32 %[v], %[a]\n\t"
               "s_waitcnt lgkmcnt(0)\n\t"
               "v_readfirstlane_b32 %[c], %[v]\n\t"
               "s_cmp_lt_u32 %[c], %[t]\n\t"
               "s_cbranch_scc1 1b\n"
               "3:"
               : [v] "=&v"(v), [c] "=&s"(c), [n] "+s"(n) : [a] "v"(w), [t] "n"(TARGET) : "scc", "memory");
}
__device__ __forceinline__ void lds_signal(LAS unsigned* w, int lane) {
  asm volatile("" ::: "memory");
  if (lane == 0) __hip_atomic_fetch_add(w, 1u, __ATOMIC_RELAXED, __HIP_MEMORY_SCOPE_WORKGROUP);
  asm volatile("" ::: "memory");
}
template <int Q> __device__ __forceinline__ void pairfrag(const v4u& r0, const v4u& r1, v4u* bf) {
#pragma unroll
  for (int d = 0; d < 8; ++d) bf[d][Q] = __builtin_amdgcn_perm(r1[d >> 1], r0[d >> 1], (d & 1) ? 0x07060302u : 0x05040100u);
}

constexpr int CAP = 8;
constexpr int TA = 200, TB = 28;
constexpr int TI1 = 27, TE = 8;
constexpr int TF = 350;
constexpr int TQ = 520;
constexpr int TBF = 650;
constexpr int TQI = 24;
constexpr int MLI = 8;
constexpr int TWD = 40, TWB = 20;
constexpr int G1D = 2, G1S = 2, G1L = 6, G1T = 16;
constexpr int G2D = 10, G2S = 1, G2L = 4, G2T = 24;
#define NT 2
#define SB() __builtin_amdgcn_sched_barrier(0)
__device__ __forceinline__ unsigned now32() { return (unsigned)__builtin_amdgcn_s_memrealtime(); }

__device__ __forceinline__ void wg_stats(const unsigned char* lds, int lane, float& M, float& L) {
  const LAS f32x4* mlp = (const LAS f32x4*)(lds + L_MLW + (lane & 3) * 64);
  const f32x4 t0 = mlp[0], t1 = mlp[1], t2 = mlp[2], t3 = mlp[3];
  M = fmaxf(fmaxf(fmaxf(t0.x, t0.z), fmaxf(t1.x, t1.z)), fmaxf(fmaxf(t2.x, t2.z), fmaxf(t3.x, t3.z)));
  L = (t0.y * __builtin_amdgcn_exp2f(t0.x - M) + t0.w * __builtin_amdgcn_exp2f(t0.z - M)) +
      (t1.y * __builtin_amdgcn_exp2f(t1.x - M) + t1.w * __builtin_amdgcn_exp2f(t1.z - M)) +
      ((t2.y * __builtin_amdgcn_exp2f(t2.x - M) + t2.w * __builtin_amdgcn_exp2f(t2.z - M)) +
       (t3.y * __builtin_amdgcn_exp2f(t3.x - M) + t3.w * __builtin_amdgcn_exp2f(t3.z - M)));
}

template <int STRIDE, int SHORT, int LONG, int THR> __device__ __forceinline__ bool gather(v4u ds, int off, v4u* r, v4u* x) {
  unsigned t, c;
  int cnt = (int)SPIN_CAP;
  u64 m0, m1, m2, m3, ma;
#pragma unroll
  for (int n = 0; n < 4; ++n) x[n] = v4u{SENT, SENT, SENT, SENT};
  asm volatile(
      "buffer_load_dwordx4 v[96:99], %[o0], %[ds], 0 offen sc1\n\t"
      "buffer_load_dwordx4 v[100:103], %[o1], %[ds], 0 offen sc1\n\t"
      "buffer_load_dwordx4 v[104:107], %[o2], %[ds], 0 offen sc1\n\t"
      "buffer_load_dwordx4 v[108:111], %[o3], %[ds], 0 offen sc1\n\t"
      "s_waitcnt vmcnt(0)\n"
      "1:\n\t"
      "v_max3_u32 %[t], v96, v97, v98\n\tv_max_u32 %[t], %[t], v99\n\tv_cmp_eq_u32 %[m0], -1, %[t]\n\t"
      "v_max3_u32 %[t], v100, v101, v102\n\tv_max_u32 %[t], %[t], v103\n\tv_cmp_eq_u32 %[m1], -1, %[t]\n\t"
      "v_max3_u32 %[t], v104, v105, v106\n\tv_max_u32 %[t], %[t], v107\n\tv_cmp_eq_u32 %[m2], -1, %[t]\n\t"
      "v_max3_u32 %[t], v108, v109, v110\n\tv_max_u32 %[t], %[t], v111\n\tv_cmp_eq_u32 %[m3], -1, %[t]\n\t"
      "s_or_b64 %[ma], %[m0], %[m1]\n\t"
      "s_or_b64 %[ma], %[ma], %[m2]\n\t"
      "s_or_b64 %[ma], %[ma], %[m3]\n\t"
      "s_cbranch_scc0 3f\n\t"
      "s_sub_u32 %[cnt], %[cnt], 1\n\t"
      "s_cbranch_scc1 3f\n\t"
      "s_mov_b64 exec, %[m0]\n\t"
      "buffer_load_dwordx4 v[112:115], %[o0], %[ds], 0 offen sc1\n\t"
      "s_mov_b64 exec, %[m1]\n\t"
      "buffer_load_dwordx4 v[116:119], %[o1], %[ds], 0 offen sc1\n\t"
      "s_mov_b64 exec, %[m2]\n\t"
      "buffer_load_dwordx4 v[120:123], %[o2], %[ds], 0 offen sc1\n\t"
      "s_mov_b64 exec, %[m3]\n\t"
      "buffer_load_dwordx4 v[124:127], %[o3], %[ds], 0 offen sc1\n\t"
      "s_mov_b64 exec, -1\n\t"
      "s_bcnt1_i32_b64 %[c], %[ma]\n\t"
      "s_cmp_gt_u32 %[c], %[thr]\n\t"
      "s_cbranch_scc0 2f\n\t"
      "s_sleep %[lng]\n"
      "2:\n\t"
      "s_sleep %[sht]\n\t"
      "s_waitcnt vmcnt(24)\n\t"
      "v_min_u32 v96, v96, v112\n\tv_min_u32 v97, v97, v113\n\tv_min_u32 v98, v98, v114\n\tv_min_u32 v99, v99, v115\n\tv_min_u32 v100, v100, v116\n\tv_min_u32 v101, v101, v117\n\tv_min_u32 v102, v102, v118\n\tv_min_u32 v103, v103, v119\n\tv_min_u32 v104, v104, v120\n\tv_min_u32 v105, v105, v121\n\tv_min_u32 v106, v106, v122\n\tv_min_u32 v107, v107, v123\n\tv_min_u32 v108, v108, v124\n\tv_min_u32 v109, v109, v125\n\tv_min_u32 v110, v110, v126\n\tv_min_u32 v111, v111, v127\n\t"
      "s_branch 1b\n"
      "3:"
      : "={v[96:99]}"(r[0]), "={v[100:103]}"(r[1]), "={v[104:107]}"(r[2]), "={v[108:111]}"(r[3]),
        "+{v[112:115]}"(x[0]), "+{v[116:119]}"(x[1]), "+{v[120:123]}"(x[2]), "+{v[124:127]}"(x[3]),
        [t] "=&v"(t), [c] "=&s"(c), [cnt] "+s"(cnt), [m0] "=&s"(m0), [m1] "=&s"(m1), [m2] "=&s"(m2), [m3] "=&s"(m3), [ma] "=&s"(ma)
      : [o0] "v"(off), [o1] "v"(off + STRIDE), [o2] "v"(off + 2 * STRIDE), [o3] "v"(off + 3 * STRIDE), [ds] "s"(ds),
        [thr] "n"(THR), [lng] "n"(LONG), [sht] "n"(SHORT)
      : "memory", "scc");
  return cnt >= 0;
}
__device__ __forceinline__ void drain(v4u* x) {
  asm volatile("s_waitcnt vmcnt(0)" :: "{v[112:115]}"(x[0]), "{v[116:119]}"(x[1]), "{v[120:123]}"(x[2]), "{v[124:127]}"(x[3]) : "memory");
}
template <int SLP> __device__ __forceinline__ void wait_clock(unsigned t0, int ticks) {
  asm volatile("s_branch 2f\n"
               "1:\n\ts_sleep %2\n"
               "2:\n\ts_memrealtime vcc\n\t"
               "s_waitcnt lgkmcnt(0)\n\t"
               "s_sub_u32 vcc_lo, vcc_lo, %0\n\t"
               "s_cmpk_lt_i32 vcc_lo, %1\n\t"
               "s_cbranch_scc1 1b" :: "s"(t0), "n"(ticks), "n"(SLP) : "vcc", "scc", "memory");
}

__global__ void __launch_bounds__(NTHR, 3) fused_decode(Params p) {
  __shared__ __attribute__((aligned(16))) unsigned char lds[L_BYTES];
  u64 t64 = __builtin_amdgcn_s_memrealtime();
  const int tid = threadIdx.x, lane = tid & 63;
  const int wave = __builtin_amdgcn_readfirstlane(tid >> 6);
  const int id = blockIdx.x;
  const int kvh = id & 7, br = id >> 7, j = (id >> 3) & 15;
  const int grp = br * 8 + kvh;
  const int lg = lane >> 4, c16 = lane & 15;
  const unsigned short *px = p.in[br * 5], *pk = p.in[br * 5 + 1], *pv = p.in[br * 5 + 2], *pwq = p.in[br * 5 + 3], *pwo = p.in[br * 5 + 4];
  unsigned short* pout = p.out;
  unsigned char* pws = p.ws;
#define ARGS() \
  const unsigned t0 = (unsigned)t64; \
  const __amdgpu_buffer_rsrc_t rs = __builtin_amdgcn_make_buffer_rsrc(pws, 0, WS_END, 0x00020000); \
  const v4u ds = {(unsigned)(size_t)pws, (unsigned)((size_t)pws >> 32), (unsigned)WS_END, 0x00020000u}
  LAS unsigned* sync = (LAS unsigned*)(lds + L_SYNC);
  const int wg = grp * 16 + j;
  unsigned tc0 = 0;
  if (wave == 2 && id < 32) {
    u64 pc;
    unsigned len;
    asm volatile("s_getpc_b64 %0\n.Lfd_here:\n\ts_mov_b32 %1, .Lfd_end-.Lfd_here" : "=s"(pc), "=s"(len));
    const __amdgpu_buffer_rsrc_t rc = __builtin_amdgcn_make_buffer_rsrc((void*)pc, 0, len, 0x00020000);
    tc0 = __builtin_amdgcn_raw_buffer_load_b32(rc, lane < 28 ? lane * 512 + 768 : OOB, (id >> 3) * 128, 0);
  }
  v2u ev;
  v4u kr[16];
  int ko, rot;
  const int w = wave - 4;
  const int kvo = (((j >> 1) * HKV + kvh) * SK + (SK - WIN) + (j & 1) * 512 + w * 64) * (DH * 2);
#define RI(i) ((((i) + rot) & 15) << 10)
  if (wave >= 4) {
    ko = kvo + (c16 >> 2) * 256 + lg * 64 + (lane & 3) * 16;
    rot = (w * 2 + j * 5 + kvh * 3 + br * 8) & 15;
    asm volatile("" : "+v"(ko), "+s"(rot));
    SB();
    const __amdgpu_buffer_rsrc_t rk = __builtin_amdgcn_make_buffer_rsrc((void*)pk, 0, NB * HKV * SK * DH * 2, 0x00020000);
    asm volatile("s_barrier" ::: "memory");
    SB();
#pragma unroll
    for (int i = 0; i < 3; ++i) { kr[i] = __builtin_amdgcn_raw_buffer_load_b128(rk, ko, RI(i), NT); SB(); }
  } else if (wave >= 2) {
    if (wave == 2) {
      if (lane < 15) sync[lane] = 0u;
    } else {
      const __amdgpu_buffer_rsrc_t rs = __builtin_amdgcn_make_buffer_rsrc(pws, 0, WS_END, 0x00020000);
      ev = __builtin_amdgcn_raw_buffer_load_b64(rs, WS_EPOCH, 0, 0);
      const v4u s4 = {SENT, SENT, SENT, SENT};
      st_sc1(rs, lane < 2 ? WS_ML + wg * 256 + lane * 16 : OOB, s4);
#pragma unroll
      for (int i = 0; i < 7; ++i) __builtin_amdgcn_raw_buffer_store_b128(s4, rs, WS_BOX + wg * BOX + lane * 16 + (i & 3) * 1024, (i >> 2) * 4096, SC1);
      SB();
    }
    BAR();
  }
  SB();
  if (wave >= 2) {
   if (wave < 4) {
    if (wave == 2) {
      __builtin_amdgcn_s_setprio(2);
      bool fail = false;
      ARGS();
      asm volatile("" :: "v"(tc0));
      lds_wait<1>(sync + S_TAGV);
      const v2u tagv = *(LAS v2u*)(sync + S_TAG);
      wait_clock<2>(t0, TF);
      for (unsigned spins = 0;; ++spins) {
        const v2u f = ld_sc1_8(rs, lane < 16 ? WS_FLAGS + (grp * 16 + lane) * 256 : OOB);
        if (__all(lane >= 16 || (f.x == tagv.x && f.y == tagv.y))) break;
        if (spins > SPIN_CAP) { fail = true; break; }
        __builtin_amdgcn_s_sleep(8);
      }
      asm volatile("" ::: "memory");
      SB();
      lds_wait<2>(sync + S_QP);
      wait_clock<2>(t0, TQ);
      v4u q[4], xq[4];
      if (!gather<BOX, TQI, 0, 64>(ds, WS_Q + (grp * 16 + lg * 4) * BOX + (j >> 1) * 256 + c16 * 16, q, xq)) fail = true;
      {
        const f32x4 s0 = __builtin_bit_cast(f32x4, q[0]) + __builtin_bit_cast(f32x4, q[1]);
        const f32x4 s1 = __builtin_bit_cast(f32x4, q[2]) + __builtin_bit_cast(f32x4, q[3]);
        *(LAS v2u*)(lds + L_QF + lg * 256 + c16 * 8) = v2u{cvtpk(s0[0], s0[1]), cvtpk(s0[2], s0[3])};
        *(LAS v2u*)(lds + L_QF + lg * 256 + 128 + c16 * 8) = v2u{cvtpk(s1[0], s1[1]), cvtpk(s1[2], s1[3])};
      }
      lds_signal(sync + S_QRDY, lane);
      SB();
      drain(xq);
      lds_wait<8>(sync + S_MLC);
      float M, L;
      wg_stats(lds, lane, M, L);
      if (lane < 4) st_sc1_8(rs, WS_ML + wg * 256 + lane * 8, v2u{nosent(__float_as_uint(M)), nosent(__float_as_uint(L))});
      v2u ml;
      for (unsigned spins = 0;; ++spins) {
        __builtin_amdgcn_s_sleep(MLI);
        ml = ld_sc1_8(rs, lane < 4 ? WS_ML + (grp * 16 + (j ^ 1)) * 256 + lane * 8 : OOB);
        if (__all(ml.x != SENT && ml.y != SENT)) break;
        if (spins > SPIN_CAP) { fail = true; break; }
      }
      const float m2 = __uint_as_float(ml.x), l2 = __uint_as_float(ml.y);
      const float Mx = fmaxf(M, m2);
      const float a = L * __builtin_amdgcn_exp2f(M - Mx), a2 = l2 * __builtin_amdgcn_exp2f(m2 - Mx);
      if (lane < 4) ((LAS float*)(sync + S_WV))[lane] = a / (a + a2);
      if (fail && lane == 0) sync[S_FAIL] = 1u;
      BAR();
    } else {
      __builtin_amdgcn_s_setprio(2);
      bool fail = false;
      ARGS();
      asm volatile("s_waitcnt vmcnt(0)" : "+v"(ev) :: "memory");
      const u64 t = (((u64)ev.y << 32) | ev.x) + 1ull;
      const v2u tagv = {(unsigned)t, (unsigned)(t >> 32)};
      if (lane == 0) {
        st_sc1_8(rs, WS_FLAGS + wg * 256, tagv);
        *(LAS v2u*)(sync + S_TAG) = tagv;
      }
      lds_signal(sync + S_TAGV, lane);
      wait_clock<2>(t0, TBF);
      for (unsigned spins = 0;; ++spins) {
        if (spins) asm volatile("s_sleep 16" ::: "memory");
        const v2u f0 = ld_sc1_8(rs, lane < 32 ? WS_FLAGS + ((br * 8 + (lane >> 2)) * 16 + (lane & 3) * 4 + (kvh >> 1)) * 256 : OOB);
        bool ok = lane >= 32 || (f0.x == tagv.x && f0.y == tagv.y);
        if (id == 0) {
#pragma unroll
          for (int i = 0; i < 4; ++i) {
            const v2u f1 = ld_sc1_8(rs, WS_FLAGS + (i * 64 + lane) * 256);
            ok = ok && f1.x == tagv.x && f1.y == tagv.y;
          }
        }
        if (__all(ok)) break;
        if (spins > SPIN_CAP) { fail = true; break; }
      }
      if (id == 0 && lane == 0) st_sc1_8(rs, WS_EPOCH, tagv);
      BAR();
      const int r = kvh * 16 + j, cq2 = r >> 5, bo = (r >> 2) & 7, c32 = r & 3;
      v4u pv[4], x2[4];
      const int poff = WS_P + ((br * 8 + (lane >> 3)) * 16 + cq2) * BOX + bo * 512 + c32 * 128 + (lane & 7) * 16;
      lds_wait<2>(sync + S_PPUB);
      fail = fail || sync[S_FAIL] != 0u;
      __builtin_amdgcn_s_sleep(G2D);
      if (!gather<4 * BOX, G2S, G2L, G2T>(ds, poff, pv, x2)) fail = true;
      f32x4 t4 = (__builtin_bit_cast(f32x4, pv[0]) + __builtin_bit_cast(f32x4, pv[1])) + (__builtin_bit_cast(f32x4, pv[2]) + __builtin_bit_cast(f32x4, pv[3]));
#pragma unroll
      for (int e = 0; e < 4; ++e) { float x = t4[e]; x += dppf<ROR8>(x); t4[e] = xrow_sum(x); }
      if (fail) t4 = f32x4{__builtin_nanf(""), __builtin_nanf(""), __builtin_nanf(""), __builtin_nanf("")};
      if (lane < 8) {
        v2u o = {cvtpk(t4[0], t4[1]), cvtpk(t4[2], t4[3])};
        __builtin_nontemporal_store(o, (GAS v2u*)(pout + bo * 1024 + br * 512 + cq2 * 128 + (c32 >> 1) * 64 + (c32 & 1) * 8 + (lane >> 1) * 16 + (lane & 1) * 4));
      }
      SB();
      drain(x2);
    }
   } else {
    ARGS();
    const int vo = kvo + lane * 16;
    const __amdgpu_buffer_rsrc_t rk = __builtin_amdgcn_make_buffer_rsrc((void*)pk, 0, NB * HKV * SK * DH * 2, 0x00020000);
    const __amdgpu_buffer_rsrc_t rv = __builtin_amdgcn_make_buffer_rsrc((void*)pv, 0, NB * HKV * SK * DH * 2, 0x00020000);
#define PACE(i) do { asm volatile("s_waitcnt vmcnt(%0)" :: "n"(CAP - 1) : "memory"); wait_clock<1>(t0, TA + TB * (i) + ((i) > TI1 ? TE * ((i) - TI1) : 0)); } while (0)
    v4u vr[16];
    asm volatile("" :: "s"(t64));
#pragma unroll
    for (int i = 3; i < 16; ++i) {
      PACE(i);
      kr[i] = __builtin_amdgcn_raw_buffer_load_b128(rk, ko, RI(i), NT);
      SB();
    }
#define VLD(i) do { PACE(16 + (i)); vr[i] = __builtin_amdgcn_raw_buffer_load_b128(rv, vo, RI(i), NT); SB(); } while (0)
    VLD(0); VLD(1); VLD(2); VLD(3); VLD(4); VLD(5); VLD(6); VLD(7); VLD(8);
    lds_wait<1>(sync + S_QRDY);
    const v4u qf = *(const LAS v4u*)(lds + L_QF + (lane & 3) * 256 + (lg * 4 + (c16 >> 2)) * 16);
    float R[4];
#define QK(rr) do { \
      f32x4 Dq[4]; \
      _Pragma("unroll") for (int t = 0; t < 4; ++t) Dq[t] = mfma16(kr[rr * 4 + t], qf, f32x4{0.f, 0.f, 0.f, 0.f}); \
      float U[4]; \
      _Pragma("unroll") for (int rho = 0; rho < 4; ++rho) { \
        float u = Dq[rho][0]; \
        u = selbank<1>(u, Dq[(rho + 1) & 3][1]); \
        u = selbank<2>(u, Dq[(rho + 2) & 3][2]); \
        u = selbank<3>(u, Dq[(rho + 3) & 3][3]); \
        U[rho] = u; \
      } \
      R[rr] = (U[0] + dppf<ROR4>(U[1])) + (dppf<ROR8>(U[2]) + dppf<ROR12>(U[3])); \
      asm volatile("" : "+v"(R[rr])); \
      SB(); } while (0)
    QK(0); QK(1);
    VLD(9);
    QK(2); QK(3);
    VLD(10);
    constexpr float C2 = 0.088388347648318440f * 1.4426950408889634f;
    {
      float m = fmaxf(fmaxf(R[0], R[1]), fmaxf(R[2], R[3]));
      m = fmaxf(m, dppf<ROR4>(m));
      m = fmaxf(m, dppf<ROR8>(m));
      m = xrow_max(m) * C2;
      float l = 0.f;
#pragma unroll
      for (int rr = 0; rr < 4; ++rr) l += __builtin_amdgcn_exp2f(fmaf(R[rr], C2, -m));
      l += dppf<ROR4>(l);
      l += dppf<ROR8>(l);
      l = xrow_sum(l);
      if (lane < 4) *(LAS f32x2*)(lds + L_MLW + (lane * 8 + w) * 8) = f32x2{m, l};
      lds_signal(sync + S_MLC, lane);
    }
    SB();
    v4u b0[8], b1[8];
    pairfrag<0>(vr[0], vr[1], b0); pairfrag<1>(vr[2], vr[3], b0); pairfrag<2>(vr[4], vr[5], b0); SB();
    VLD(11); pairfrag<3>(vr[6], vr[7], b0); SB();
    lds_wait<8>(sync + S_MLC);
    v4u pa[2];
    {
      float M, L;
      wg_stats(lds, lane, M, L);
      const float inv = 1.0f / L;
#pragma unroll
      for (int rr = 0; rr < 4; ++rr) {
        const float pr = __builtin_amdgcn_exp2f(fmaf(R[rr], C2, -M)) * inv;
        const unsigned x = cvtpk(pr, dppf<SHL4>(pr));
        pa[rr >> 1][(rr & 1) * 2] = x;
        pa[rr >> 1][(rr & 1) * 2 + 1] = dppu<SHL8>(x);
      }
      asm volatile("" : "+v"(pa[0]), "+v"(pa[1]));
    }
    SB();
    VLD(12);
    f32x4 acc[8];
#pragma unroll
    for (int d = 0; d < 8; ++d) acc[d] = mfma16(pa[0], b0[d], f32x4{0.f, 0.f, 0.f, 0.f});
    SB();
    VLD(13); pairfrag<0>(vr[8], vr[9], b1); SB();
    VLD(14);
    VLD(15);
    lds_signal(sync + S_VISS, lane);
    SB();
    pairfrag<1>(vr[10], vr[11], b1); SB();
    pairfrag<2>(vr[12], vr[13], b1); SB();
    pairfrag<3>(vr[14], vr[15], b1);
#pragma unroll
    for (int d = 0; d < 8; ++d) acc[d] = mfma16(pa[1], b1[d], acc[d]);
    if (lane < 16) {
      LAS float* op = (LAS float*)(lds + L_OPART) + w * 512 + lane * 8;
#pragma unroll
      for (int h = 0; h < 4; ++h) {
        *(LAS f32x4*)(op + h * 128) = f32x4{acc[0][h], acc[1][h], acc[2][h], acc[3][h]};
        *(LAS f32x4*)(op + h * 128 + 4) = f32x4{acc[4][h], acc[5][h], acc[6][h], acc[7][h]};
      }
    }
    BAR();
    if (w < 2) {
      const int e = w * 64 + lane;
      const LAS f32x4* op = (const LAS f32x4*)(lds + L_OPART) + e;
      f32x4 o = ((op[0] + op[128]) + (op[256] + op[384])) + ((op[512] + op[640]) + (op[768] + op[896]));
      o *= ((const LAS float*)(sync + S_WV))[e >> 5];
      const unsigned u0 = nosent(cvtpk(o[0], o[1])), u1 = nosent(cvtpk(o[2], o[3]));
      const v4u ou = {u0, u1, dppu<0xF5>(u0), dppu<0xF5>(u1)};
      if (!(lane & 1)) st_sc1(rs, WS_O + wg * BOX + e * 8, ou);
    }
   }
  } else {
      const int pw = wave;
      __builtin_amdgcn_s_setprio(2);
      bool fail = false;
      ARGS();
      const int rl = c16 >> 3, c8 = lane & 7;
      const int r0 = 256 * (j & 1) + 128 * pw + 32 * rl + 8 * lg;
      int qo = r0 * (HQ * DH * 2) + kvh * 1024 + (j >> 1) * 128 + c8 * 16, xo = c8 * (DM * 2) + r0 * 2;
      asm volatile("" : "+v"(qo), "+v"(xo));
      SB();
      const __amdgpu_buffer_rsrc_t rq = __builtin_amdgcn_make_buffer_rsrc((void*)pwq, 0, DM * HQ * DH * 2, 0x00020000);
      v4u w[16];
#pragma unroll
      for (int i = 0; i < 16; ++i) { w[i] = __builtin_amdgcn_raw_buffer_load_b128(rq, qo, ((i & 7) + (i >> 3) * 64) * (HQ * DH * 2), NT); SB(); }
      const __amdgpu_buffer_rsrc_t rx = __builtin_amdgcn_make_buffer_rsrc((void*)px, 0, NB * DM * 2, 0x00020000);
      const v4u xa = __builtin_amdgcn_raw_buffer_load_b128(rx, xo, 0, 0);
      const v4u xb = __builtin_amdgcn_raw_buffer_load_b128(rx, xo + 128, 0, 0);
      SB();
      asm volatile("s_barrier" ::: "memory");
      SB();
      asm volatile("" :: "s"(t64));
      f32x4 a0[8];
#define QSTEP(D) { const v4u bfr = colfrag<D>(w); a0[D] = mfma16(xa, bfr, f32x4{0.f, 0.f, 0.f, 0.f}); }
      QSTEP(0) QSTEP(1) QSTEP(2) QSTEP(3) QSTEP(4) QSTEP(5) QSTEP(6) QSTEP(7)
#undef QSTEP
#define QSTEP(D) { const v4u bfr = colfrag<D>(w + 8); a0[D] = mfma16(xb, bfr, a0[D]); }
      QSTEP(0) QSTEP(1) QSTEP(2) QSTEP(3) QSTEP(4) QSTEP(5) QSTEP(6) QSTEP(7)
#undef QSTEP
      if (rl == (lg >> 1)) {
        LAS float* qp = (LAS float*)(lds + L_QPART) + (pw * 2 + rl) * 512 + (lg & 1) * 256 + c8 * 4;
#pragma unroll
        for (int v = 0; v < 4; ++v) {
          *(LAS f32x4*)(qp + v * 64) = f32x4{a0[0][v], a0[1][v], a0[2][v], a0[3][v]};
          *(LAS f32x4*)(qp + v * 64 + 32) = f32x4{a0[4][v], a0[5][v], a0[6][v], a0[7][v]};
        }
      }
      lds_signal(sync + S_QP, lane);
      lds_wait<2>(sync + S_QP);
      const LAS f32x4* qp = (const LAS f32x4*)(lds + L_QPART + (tid >> 4) * 256 + (tid & 1) * 128 + ((tid >> 1) & 7) * 16);
      const f32x4 s = (qp[0] + qp[128]) + (qp[256] + qp[384]);
      v4u q4 = __builtin_bit_cast(v4u, s);
      q4.x = nosent(q4.x); q4.y = nosent(q4.y); q4.z = nosent(q4.z); q4.w = nosent(q4.w);
      lds_wait<1>(sync + S_TAGV);
      st_sc1(rs, WS_Q + wg * BOX + tid * 16, q4);
      const int hh = j >> 2, cq = j & 3;
      const __amdgpu_buffer_rsrc_t rw = __builtin_amdgcn_make_buffer_rsrc((void*)(pwo + (size_t)((kvh * 4 + hh) * 128) * DM + cq * 128 + pw * 64), 0, 128 * DM * 2 - 896, 0x00020000);
      const int wof = lg * 8 * DM * 2 + c16 * 8;
      lds_wait<8>(sync + S_VISS);
      v2u u[32];
      {
        const unsigned t1 = now32();
#pragma unroll
        for (int i = 0; i < 32; ++i) {
          if (!(i & 7)) wait_clock<1>(t1, TWD + TWB * (i >> 3));
          u[i] = __builtin_amdgcn_raw_buffer_load_b64(rw, wof + (i & 3) * DM * 2, (i >> 3) * 32 * DM * 2 + ((i >> 2) & 1) * 4 * DM * 2, NT);
          SB();
        }
      }
      BAR();
      SB();
      v4u af[4][4];
#pragma unroll
      for (int kb = 0; kb < 4; ++kb) {
#pragma unroll
        for (int d = 0; d < 4; ++d) {
#pragma unroll
          for (int q = 0; q < 4; ++q) af[kb][d][q] = __builtin_amdgcn_perm(u[kb * 8 + 2 * q + 1][d >> 1], u[kb * 8 + 2 * q][d >> 1], (d & 1) ? 0x07060302u : 0x05040100u);
          asm volatile("" : "+v"(af[kb][d]));
        }
        SB();
      }
      const int jp = 2 * (lane & 7) + ((lane >> 3) & 1);
      v4u ob[4], x1[4];
      __builtin_amdgcn_s_sleep(G1D);
      if (!gather<64, G1S, G1L, G1T>(ds, WS_O + (grp * 16 + jp) * BOX + hh * 256 + lg * 16, ob, x1)) fail = true;
      f32x4 acc[4];
#pragma unroll
      for (int d = 0; d < 4; ++d) acc[d] = mfma16(af[0][d], ob[0], f32x4{0.f, 0.f, 0.f, 0.f});
#pragma unroll
      for (int kb = 1; kb < 4; ++kb)
#pragma unroll
        for (int d = 0; d < 4; ++d) acc[d] = mfma16(af[kb][d], ob[kb], acc[d]);
      const bool hi = (lane & 8) != 0;
      const int pof = WS_P + wg * BOX + (lane & 7) * 512 + pw * 256 + (lane >> 3) * 16;
#pragma unroll
      for (int k = 0; k < 2; ++k) {
        v4u ou;
#pragma unroll
        for (int d = 0; d < 4; ++d) {
          float e0 = acc[d][2 * k], e1 = acc[d][2 * k + 1];
          e0 += dppf<ROR8>(e0);
          e1 += dppf<ROR8>(e1);
          asm volatile("" : "+v"(e0), "+v"(e1));
          ou[d] = nosent(__float_as_uint(hi ? e1 : e0));
        }
        st_sc1(rs, pof + k * 128, ou);
      }
      if (fail && lane == 0) sync[S_FAIL] = 1u;
      lds_signal(sync + S_PPUB, lane);
      SB();
      drain(x1);
  }
  asm volatile(".Lfd_end:");
}

}

extern "C" void kernel_launch(void* const* d_in, const int* in_sizes, int n_in, void* d_out, int out_size, void* d_ws, size_t ws_size,
                              hipStream_t stream) {
  Params p;
  for (int i = 0; i < 10; ++i) p.in[i] = (const unsigned short*)d_in[i];
  p.out = (unsigned short*)d_out;
  p.ws = (unsigned char*)d_ws;
  fused_decode<<<NWG, NTHR, 0, stream>>>(p);
}
```
